```python
import torch

def _softmax(x, axis=-1):
    return torch.softmax(x, dim=axis)
def _gelu(x):
    return torch.nn.functional.gelu(x, approximate="tanh")
def _silu(x):
    return torch.nn.functional.silu(x)
def _sigmoid(x):
    return torch.sigmoid(x)
def _layernorm(x, axis=-1, eps=1e-5):
    m = x.mean(dim=axis, keepdim=True)
    v = x.var(dim=axis, keepdim=True, unbiased=False)
    return (x - m) / torch.sqrt(v + eps)
def _scatter_add(tgt, idx, src, axis=0):
    return tgt.clone().index_add_(axis, idx, src)
def _segment_sum(src, seg, n):
    out = torch.zeros((n,) + src.shape[1:], dtype=src.dtype)
    return out.index_add_(0, seg, src)
def _pad(x, pw):
    flat = tuple(v for pair in reversed(pw) for v in pair)
    return torch.nn.functional.pad(x, flat)

kernel_name = 'sequential_preLN__ssm_conv_s4d__mixer_token_mlp__prompt'

def setup_inputs():

    _w = (lambda a: (lambda t: t.to(torch.bfloat16) if t.is_floating_point() else t)(torch.as_tensor(a)))
    def _A_setup():
        Bsz, L, D, N = 1, 2048, 640, 192
        return {
            "u": _w((lambda *s: torch.randn(*s, dtype=torch.bfloat16) * 0.05)(Bsz, L, D)),
            "log_A_real": _w((lambda *s: torch.randn(*s, dtype=torch.bfloat16) * 6.4)(D, N) * 0.5),
            "A_imag": _w((lambda *s: torch.randn(*s, dtype=torch.bfloat16) * 0.05)(D, N) * 3.14),
            "C_real": _w((lambda *s: torch.randn(*s, dtype=torch.bfloat16) * 0.05)(D, N) / N ** 0.5),
            "C_imag": _w((lambda *s: torch.randn(*s, dtype=torch.bfloat16) * 0.05)(D, N) / N ** 0.5),
            "log_dt": _w((lambda *s: torch.randn(*s, dtype=torch.bfloat16) * 0.05)(D) * 0.5 - 4.0),
            "D_skip": _w((lambda *s: torch.randn(*s, dtype=torch.bfloat16) * 0.05)(D)),
        }
    _A_in = _A_setup()

    def _B_setup():
        B, S, D, F = 1, 2048, 640, 1536
        return {
            "x": _w((lambda *s: torch.randn(*s, dtype=torch.bfloat16) * 0.05)(B, S, D)),
            "w1": _w((lambda *s: torch.randn(*s, dtype=torch.bfloat16) * 0.4)(S, F) * (S ** -0.5)),
            "b1": _w((lambda *s: torch.randn(*s, dtype=torch.bfloat16) * 12.8)(F) * 0.02),
            "w2": _w((lambda *s: torch.randn(*s, dtype=torch.bfloat16) * 0.2)(F, S) * (F ** -0.5)),
            "b2": _w((lambda *s: torch.randn(*s, dtype=torch.bfloat16) * 3.2)(S) * 0.02),
        }
    _B_in = _B_setup()

    return {
        "A_u": _A_in["u"],
        "A_log_A_real": _A_in["log_A_real"],
        "A_A_imag": _A_in["A_imag"],
        "A_C_real": _A_in["C_real"],
        "A_C_imag": _A_in["C_imag"],
        "A_log_dt": _A_in["log_dt"],
        "A_D_skip": _A_in["D_skip"],
        "B_x": _B_in["x"],
        "B_w1": _B_in["w1"],
        "B_b1": _B_in["b1"],
        "B_w2": _B_in["w2"],
        "B_b2": _B_in["b2"],
    }

def reference(A_u, A_log_A_real, A_A_imag, A_C_real, A_C_imag, A_log_dt, A_D_skip, B_x, B_w1, B_b1, B_w2, B_b2):
    def _A_ref(u, log_A_real, A_imag, C_real, C_imag, log_dt, D_skip):
        L = u.shape[1]
        dt = torch.exp(log_dt)
        A = -torch.exp(log_A_real) + 1j * A_imag
        C = (C_real + 1j * C_imag) * dt[:, None]
        pos = (lambda x, dt: x.to(dt))(torch.arange(L), log_dt.dtype)
        P = torch.exp((A * dt[:, None])[:, :, None] * pos[None, None, :])
        K = 2.0 * torch.einsum("dn,dnl->dl", C, P).real
        L2 = 2 * L
        u_t = (lambda x, *d: x.permute(*d))(u, 0, 2, 1)
        Kf = (lambda x, *a, **k: torch.fft.rfft(x.float(), *a, **k))(K, L2)
        Uf = (lambda x, *a, **k: torch.fft.rfft(x.float(), *a, **k))(u_t, L2)
        y = (lambda x, *a, **k: torch.fft.irfft(x, *a, **k).to(torch.bfloat16))(Uf * Kf[None, :, :], L2)[:, :, :L]
        y = (lambda x, *d: x.permute(*d))(y, 0, 2, 1)
        return y + u * D_skip[None, None, :]

    def _B_ref(x, w1, b1, w2, b2):
        B, S, D = x.shape
        h = _layernorm(x, axis=-1)
        u = torch.einsum("bsd,sf->bdf", h, w1) + b1
        u = _gelu(u)
        y = torch.einsum("bdf,fs->bds", u, w2) + b2
        return (lambda x, *d: x.permute(*d))(y, 0, 2, 1)

    x = A_u
    h = x + _A_ref(_layernorm(x, axis=-1), A_log_A_real, A_A_imag, A_C_real, A_C_imag, A_log_dt, A_D_skip)
    h2 = h + _B_ref(_layernorm(h, axis=-1), B_w1, B_b1, B_w2, B_b2)
    return h2
```

```cpp
#include <hip/hip_runtime.h>
#include <hip/hip_bf16.h>
#include <stdint.h>

typedef unsigned short u16;
typedef __attribute__((ext_vector_type(8))) short bf16x8;
typedef __attribute__((ext_vector_type(4))) short bf16x4;
typedef __attribute__((ext_vector_type(4))) float f32x4;
typedef __attribute__((ext_vector_type(16))) float f32x16;
typedef __attribute__((address_space(3))) bf16x4 lds_bf16x4;

constexpr int SL = 2048;
constexpr int DM = 640;
constexpr int NM = 192;
constexpr int FF = 1536;

__device__ __forceinline__ float bf2f(u16 v) { return __uint_as_float(((unsigned)v) << 16); }
__device__ __forceinline__ u16 f2bf(float f) { __hip_bfloat16 b = __float2bfloat16(f); return __builtin_bit_cast(u16, b); }
__device__ __forceinline__ float rbf(float f) { return bf2f(f2bf(f)); }

__device__ __forceinline__ float wave_sum(float v) {
#pragma unroll
  for (int o = 32; o >= 1; o >>= 1) v += __shfl_xor(v, o, 64);
  return v;
}

__global__ __launch_bounds__(256) void prep_kernel(
    const u16* __restrict__ x, const u16* __restrict__ logA, const u16* __restrict__ Aim,
    const u16* __restrict__ Cre, const u16* __restrict__ Cim, const u16* __restrict__ logdt,
    float* __restrict__ statm, float* __restrict__ statd, u16* __restrict__ Khi, u16* __restrict__ Klo) {
  const int tid = threadIdx.x, w = tid >> 6, lane = tid & 63;
  if (blockIdx.x >= DM) {
    const int r0 = (blockIdx.x - DM) * 16 + w * 4;
#pragma unroll
    for (int rr = 0; rr < 4; ++rr) {
      const int row = r0 + rr;
      const unsigned* xr = (const unsigned*)(x + (size_t)row * DM);
      float v[10];
#pragma unroll
      for (int k = 0; k < 5; ++k) {
        unsigned p = xr[lane + 64 * k];
        v[2 * k] = bf2f((u16)(p & 0xffff)); v[2 * k + 1] = bf2f((u16)(p >> 16));
      }
      float s = 0.f;
#pragma unroll
      for (int k = 0; k < 10; ++k) s += v[k];
      s = wave_sum(s);
      const float mean = s * (1.0f / DM);
      float q = 0.f;
#pragma unroll
      for (int k = 0; k < 10; ++k) { float dlt = v[k] - mean; q += dlt * dlt; }
      q = wave_sum(q);
      const float var = q * (1.0f / DM);
      if (lane == 0) {
        statm[row] = rbf(mean);
        statd[row] = rbf(sqrtf(rbf(rbf(var) + 1e-5f)));
      }
    }
    return;
  }
  __shared__ float4 prm[NM];
  __shared__ float Kq[648];
  const int d = blockIdx.x;
  if (tid < NM) {
    const float dt = rbf(expf(bf2f(logdt[d])));
    const float ar = -rbf(expf(bf2f(logA[d * NM + tid])));
    const float ai = bf2f(Aim[d * NM + tid]);
    const float cr = bf2f(Cre[d * NM + tid]) * dt;
    const float ci = bf2f(Cim[d * NM + tid]) * dt;
    prm[tid] = make_float4(ar * dt, ai * dt, 2.0f * cr, 2.0f * ci);
  }
  __syncthreads();
  {
    const int i = lane & 15, kq = lane >> 4;
    float pc, pf;
    if (w == 0) { pc = 16.0f * i; pf = (float)i; }
    else { const int step = 1 << w; pc = (float)((128 << w) + 16 * step * i); pf = (float)(step * i); }
    f32x4 acc = {0.f, 0.f, 0.f, 0.f};
    const float inv2pi = 0.15915494309189535f;
#pragma unroll 2
    for (int n0 = 0; n0 < NM; n0 += 4) {
      const float4 P = prm[n0 + kq];
      float ec = __expf(P.x * pc);
      float rc = (P.y * pc) * inv2pi; rc -= rintf(rc);
      float sc = __builtin_amdgcn_sinf(rc), cc = __builtin_amdgcn_cosf(rc);
      float gr = ec * (P.z * cc - P.w * sc);
      float gi = ec * (P.z * sc + P.w * cc);
      float ef = __expf(P.x * pf);
      float rf = (P.y * pf) * inv2pi; rf -= rintf(rf);
      float sf = __builtin_amdgcn_sinf(rf), cf = __builtin_amdgcn_cosf(rf);
      float fr = ef * cf, fi = ef * sf;
      acc = __builtin_amdgcn_mfma_f32_16x16x4f32(gr, fr, acc, 0, 0, 0);
      acc = __builtin_amdgcn_mfma_f32_16x16x4f32(-gi, fi, acc, 0, 0, 0);
    }
#pragma unroll
    for (int r = 0; r < 4; ++r) {
      const int row = 4 * kq + r;
      if (w == 0) Kq[16 * row + i] = acc[r];
      else if (row < 8) Kq[256 + 128 * (w - 1) + 16 * row + i] = acc[r];
      else if (w == 3 && row == 8 && i == 0) Kq[640] = acc[r];
    }
  }
  __syncthreads();
#pragma unroll
  for (int k = 0; k < 8; ++k) {
    const int l = tid + 256 * k;
    const int p = (int)rbf((float)l);
    int idx;
    if (p < 256) idx = p;
    else if (p < 512) idx = 256 + ((p - 256) >> 1);
    else if (p < 1024) idx = 384 + ((p - 512) >> 2);
    else idx = 512 + ((p - 1024) >> 3);
    const float v = Kq[idx];
    const u16 hi = f2bf(v);
    const u16 lo = f2bf(v - bf2f(hi));
    Khi[(size_t)d * SL + l] = hi;
    Klo[(size_t)d * SL + l] = lo;
  }
}

constexpr int CV_UC = 0;
constexpr int CV_KH = 8 * 4352;
constexpr int CV_KL = CV_KH + 7680;
constexpr int CV_UR = CV_KL + 7680;
constexpr int CV_TOTAL = CV_UR + 4160;
__device__ __constant__ int kCopyOff[8] = {0, 1, 5, 9, 13, 5, 9, 13};

__global__ __launch_bounds__(256) void conv_kernel(
    const u16* __restrict__ x, const float* __restrict__ statm, const float* __restrict__ statd,
    const u16* __restrict__ Khi, const u16* __restrict__ Klo, const u16* __restrict__ Dskip,
    u16* __restrict__ aT) {
  __shared__ __attribute__((aligned(16))) unsigned char sm[CV_TOTAL];
  const int tid = threadIdx.x, w = tid >> 6, lane = tid & 63;
  const int d = blockIdx.x;
  u16* ur = (u16*)(sm + CV_UR);
  float ukeep[8];
#pragma unroll
  for (int k = 0; k < 8; ++k) {
    const int l = tid + 256 * k;
    const float xv = bf2f(x[(size_t)l * DM + d]);
    const float uv = rbf(rbf(xv - statm[l]) / statd[l]);
    ukeep[k] = uv;
    ur[2048 - l] = f2bf(uv);
  }
  if (tid < 32) { ur[2049 + tid] = 0; if (tid == 31) ur[0] = 0; }
  {
    const uint4 vh = *(const uint4*)(Khi + (size_t)d * SL + tid * 8);
    const uint4 vl = *(const uint4*)(Klo + (size_t)d * SL + tid * 8);
    const int b = tid >> 2, part = tid & 3;
    *(uint4*)(sm + CV_KH + (b + 32) * 80 + part * 16) = vh;
    *(uint4*)(sm + CV_KL + (b + 32) * 80 + part * 16) = vl;
    if (tid < 160) {
      *(uint4*)(sm + CV_KH + tid * 16) = make_uint4(0, 0, 0, 0);
      *(uint4*)(sm + CV_KL + tid * 16) = make_uint4(0, 0, 0, 0);
    }
  }
  __syncthreads();
  for (int it = tid; it < 8 * 260; it += 256) {
    const int t = it & 7, q = it >> 3;
    bf16x8 v;
#pragma unroll
    for (int e = 0; e < 8; ++e) { int idx = 8 * q + t + e; v[e] = (idx < 2080) ? (short)ur[idx] : (short)0; }
    *(bf16x8*)(sm + CV_UC + t * 4352 + 16 * (kCopyOff[t] + q)) = v;
  }
  __syncthreads();
  const int r = lane & 31, h = lane >> 5;
  const int t = (8 - (r & 7)) & 7;
  const int aBase = CV_UC + t * 4352 + 16 * (kCopyOff[t] - (r >> 3) - ((r & 7) ? 1 : 0) + h);
  f32x16 acc0 = {}, acc1 = {};
  for (int dl = w; dl < 64; dl += 4) {
#pragma unroll
    for (int s = 0; s < 2; ++s) {
      const bf16x8 af = *(const bf16x8*)(sm + aBase + 16 * (256 - 4 * dl + 2 * s));
      const int kb1 = (64 + r - dl) * 80 + 32 * s + 16 * h;
      const bf16x8 bh1 = *(const bf16x8*)(sm + CV_KH + kb1);
      const bf16x8 bl1 = *(const bf16x8*)(sm + CV_KL + kb1);
      acc1 = __builtin_amdgcn_mfma_f32_32x32x16_bf16(af, bh1, acc1, 0, 0, 0);
      acc1 = __builtin_amdgcn_mfma_f32_32x32x16_bf16(af, bl1, acc1, 0, 0, 0);
      if (dl < 32) {
        const int kb0 = (32 + r - dl) * 80 + 32 * s + 16 * h;
        const bf16x8 bh0 = *(const bf16x8*)(sm + CV_KH + kb0);
        const bf16x8 bl0 = *(const bf16x8*)(sm + CV_KL + kb0);
        acc0 = __builtin_amdgcn_mfma_f32_32x32x16_bf16(af, bh0, acc0, 0, 0, 0);
        acc0 = __builtin_amdgcn_mfma_f32_32x32x16_bf16(af, bl0, acc0, 0, 0, 0);
      }
    }
  }
  __syncthreads();
  float* red = (float*)(sm + CV_UC);
#pragma unroll
  for (int i = 0; i < 16; ++i) {
    const int row = (i & 3) + 8 * (i >> 2) + 4 * h;
    red[(w * 2 + 0) * 1056 + r * 33 + row] = acc0[i];
    red[(w * 2 + 1) * 1056 + r * 33 + row] = acc1[i];
  }
  __syncthreads();
  const float dsk = bf2f(Dskip[d]);
#pragma unroll
  for (int k = 0; k < 8; ++k) {
    const int l = tid + 256 * k;
    const int a = l >> 5, rr = l & 31;
    const int tile = a >> 5, col = a & 31;
    float y = 0.f;
#pragma unroll
    for (int ww = 0; ww < 4; ++ww) y += red[(ww * 2 + tile) * 1056 + col * 33 + rr];
    const float av = rbf(y) + rbf(ukeep[k] * dsk);
    aT[(size_t)d * SL + l] = f2bf(av);
  }
}

__global__ __launch_bounds__(256) void ln2_kernel(
    const u16* __restrict__ x, const u16* __restrict__ aT, u16* __restrict__ hbuf, u16* __restrict__ hn) {
  __shared__ __attribute__((aligned(16))) u16 tile[DM * 18];
  const int tid = threadIdx.x, w = tid >> 6, lane = tid & 63;
  const int l0 = blockIdx.x * 16;
  for (int it = tid; it < DM * 2; it += 256) {
    const int dd = it >> 1, part = it & 1;
    const uint4 v = *(const uint4*)(aT + (size_t)dd * SL + l0 + part * 8);
    unsigned* dst = (unsigned*)(tile + dd * 18 + part * 8);
    dst[0] = v.x; dst[1] = v.y; dst[2] = v.z; dst[3] = v.w;
  }
  __syncthreads();
#pragma unroll
  for (int rr = 0; rr < 4; ++rr) {
    const int rl = w * 4 + rr, row = l0 + rl;
    const unsigned* xr = (const unsigned*)(x + (size_t)row * DM);
    float v[10];
#pragma unroll
    for (int k = 0; k < 5; ++k) {
      const unsigned p = xr[lane + 64 * k];
      const int d0 = 2 * (lane + 64 * k);
      v[2 * k] = rbf(bf2f((u16)(p & 0xffff)) + bf2f(tile[d0 * 18 + rl]));
      v[2 * k + 1] = rbf(bf2f((u16)(p >> 16)) + bf2f(tile[(d0 + 1) * 18 + rl]));
    }
    unsigned* hr = (unsigned*)(hbuf + (size_t)row * DM);
#pragma unroll
    for (int k = 0; k < 5; ++k) hr[lane + 64 * k] = (unsigned)f2bf(v[2 * k]) | ((unsigned)f2bf(v[2 * k + 1]) << 16);
#pragma unroll
    for (int pass = 0; pass < 2; ++pass) {
      float s = 0.f;
#pragma unroll
      for (int k = 0; k < 10; ++k) s += v[k];
      s = wave_sum(s);
      const float mean = s * (1.0f / DM);
      float q = 0.f;
#pragma unroll
      for (int k = 0; k < 10; ++k) { float dlt = v[k] - mean; q += dlt * dlt; }
      q = wave_sum(q);
      const float mb = rbf(mean);
      const float den = rbf(sqrtf(rbf(rbf(q * (1.0f / DM)) + 1e-5f)));
#pragma unroll
      for (int k = 0; k < 10; ++k) v[k] = rbf(rbf(v[k] - mb) / den);
    }
    unsigned* nr = (unsigned*)(hn + (size_t)row * DM);
#pragma unroll
    for (int k = 0; k < 5; ++k) nr[lane + 64 * k] = (unsigned)f2bf(v[2 * k]) | ((unsigned)f2bf(v[2 * k + 1]) << 16);
  }
}

#define WAIT_V(n) asm volatile("s_waitcnt vmcnt(" #n ")" ::: "memory")
__device__ __forceinline__ void glds16(const void* g, void* l) {
  __builtin_amdgcn_global_load_lds((const unsigned*)g, (unsigned*)l, 16, 0, 0);
}
__device__ __forceinline__ bf16x4 trread(const unsigned char* p) {
  return __builtin_amdgcn_ds_read_tr16_b64_v4i16((lds_bf16x4*)p);
}
__device__ __forceinline__ bf16x8 cat8(bf16x4 a, bf16x4 b) {
  bf16x8 r; r[0] = a[0]; r[1] = a[1]; r[2] = a[2]; r[3] = a[3]; r[4] = b[0]; r[5] = b[1]; r[6] = b[2]; r[7] = b[3]; return r;
}
__device__ __forceinline__ float gelu_tanh(float v) {
  const float k0 = 0.7978845608028654f, k1 = 0.044715f;
  float inner = k0 * (v + k1 * v * v * v);
  return 0.5f * v * (1.0f + tanhf(inner));
}

__global__ __launch_bounds__(256, 1) void gemm1_kernel(
    const u16* __restrict__ hn, const u16* __restrict__ w1, const u16* __restrict__ b1, u16* __restrict__ g) {
  __shared__ __attribute__((aligned(16))) unsigned char sm[4 * 3 * 8192];
  const int tid = threadIdx.x, w = tid >> 6, lane = tid & 63;
  const int xg = blockIdx.x & 7, li = blockIdx.x >> 3;
  const int nt = xg * 3 + li / 10, mt = li % 10;
  const int m0 = mt * 64, n0 = nt * 64;
  unsigned char* ring = sm + w * 3 * 8192;
  const int kbase = w * 512;
  const int srow = lane >> 3, sch = lane & 7;
  auto stage = [&](int it) {
    unsigned char* dst = ring + (it % 3) * 8192;
    const int k0 = kbase + it * 32;
#pragma unroll
    for (int i = 0; i < 4; ++i) {
      const int row = 8 * i + srow;
      const int ch = sch ^ (((row >> 1) & 1) << 2);
      glds16(hn + (size_t)(k0 + row) * DM + m0 + ch * 8, dst + i * 1024);
    }
#pragma unroll
    for (int i = 0; i < 4; ++i) {
      const int row = 8 * i + srow;
      const int ch = sch ^ (((row >> 1) & 1) << 2);
      glds16(w1 + (size_t)(k0 + row) * FF + n0 + ch * 8, dst + 4096 + i * 1024);
    }
  };
  f32x16 acc[2][2] = {};
  const int gq = lane >> 4, q = (lane & 15) >> 2, p = lane & 3;
  auto compute = [&](int it) {
    const unsigned char* buf = ring + (it % 3) * 8192;
#pragma unroll
    for (int ks = 0; ks < 2; ++ks) {
      bf16x8 af[2], bfr[2];
#pragma unroll
      for (int mi = 0; mi < 2; ++mi) {
        bf16x4 part[2];
#pragma unroll
        for (int t = 0; t < 2; ++t) {
          const int row = 16 * ks + 8 * (gq >> 1) + 4 * t + q;
          const int ch = (4 * mi + 2 * (gq & 1) + (p >> 1)) ^ (((row >> 1) & 1) << 2);
          part[t] = trread(buf + row * 128 + ch * 16 + 8 * (p & 1));
        }
        af[mi] = cat8(part[0], part[1]);
#pragma unroll
        for (int t = 0; t < 2; ++t) {
          const int row = 16 * ks + 8 * (gq >> 1) + 4 * t + q;
          const int ch = (4 * mi + 2 * (gq & 1) + (p >> 1)) ^ (((row >> 1) & 1) << 2);
          part[t] = trread(buf + 4096 + row * 128 + ch * 16 + 8 * (p & 1));
        }
        bfr[mi] = cat8(part[0], part[1]);
      }
#pragma unroll
      for (int mi = 0; mi < 2; ++mi)
#pragma unroll
        for (int ni = 0; ni < 2; ++ni)
          acc[mi][ni] = __builtin_amdgcn_mfma_f32_32x32x16_bf16(af[mi], bfr[ni], acc[mi][ni], 0, 0, 0);
    }
  };
  stage(0); stage(1);
  for (int it = 0; it < 14; ++it) {
    stage(it + 2);
    WAIT_V(16);
    compute(it);
  }
  WAIT_V(8); compute(14);
  WAIT_V(0); compute(15);
  __syncthreads();
  float* red = (float*)sm;
  {
    const int h = lane >> 5, c = lane & 31;
#pragma unroll
    for (int mi = 0; mi < 2; ++mi)
#pragma unroll
      for (int ni = 0; ni < 2; ++ni)
#pragma unroll
        for (int i = 0; i < 16; ++i) {
          const int m = 32 * mi + (i & 3) + 8 * (i >> 2) + 4 * h;
          red[w * 4160 + m * 65 + 32 * ni + c] = acc[mi][ni][i];
        }
  }
  __syncthreads();
  {
    const int m = tid >> 2, nb = (tid & 3) * 16;
    u16 o[16];
#pragma unroll
    for (int j = 0; j < 16; ++j) {
      float s = 0.f;
#pragma unroll
      for (int ww = 0; ww < 4; ++ww) s += red[ww * 4160 + m * 65 + nb + j];
      const float v = rbf(rbf(s) + bf2f(b1[n0 + nb + j]));
      o[j] = f2bf(gelu_tanh(v));
    }
    uint4* dst = (uint4*)(g + (size_t)(m0 + m) * FF + n0 + nb);
    uint4 v0, v1;
    v0.x = o[0] | ((unsigned)o[1] << 16); v0.y = o[2] | ((unsigned)o[3] << 16); v0.z = o[4] | ((unsigned)o[5] << 16); v0.w = o[6] | ((unsigned)o[7] << 16);
    v1.x = o[8] | ((unsigned)o[9] << 16); v1.y = o[10] | ((unsigned)o[11] << 16); v1.z = o[12] | ((unsigned)o[13] << 16); v1.w = o[14] | ((unsigned)o[15] << 16);
    dst[0] = v0; dst[1] = v1;
  }
}

__global__ __launch_bounds__(256, 1) void gemm2_kernel(
    const u16* __restrict__ w2, const u16* __restrict__ g, const u16* __restrict__ b2,
    const u16* __restrict__ hbuf, u16* __restrict__ out) {
  __shared__ __attribute__((aligned(16))) unsigned char sm[4 * 3 * 9216];
  const int tid = threadIdx.x, w = tid >> 6, lane = tid & 63;
  const int xg = blockIdx.x & 7, li = blockIdx.x >> 3;
  const int st = xg * 4 + (li >> 3), dtile = li & 7;
  const int s0 = st * 64, d0 = dtile * 80;
  unsigned char* ring = sm + w * 3 * 9216;
  const int kbase = w * 384;
  auto stage = [&](int it) {
    unsigned char* dst = ring + (it % 3) * 9216;
    const int k0 = kbase + it * 32;
#pragma unroll
    for (int i = 0; i < 4; ++i) {
      const int row = 8 * i + (lane >> 3);
      const int ch = (lane & 7) ^ ((((row >> 1) & 1) << 1) | (((row >> 3) & 1) << 2));
      glds16(w2 + (size_t)(k0 + row) * SL + s0 + ch * 8, dst + i * 1024);
    }
#pragma unroll
    for (int i = 0; i < 5; ++i) {
      const int row = 16 * i + (lane >> 2);
      const int ch = (lane & 3) ^ ((row >> 1) & 3);
      glds16(g + (size_t)(d0 + row) * FF + k0 + ch * 8, dst + 4096 + i * 1024);
    }
  };
  f32x4 acc[4][5] = {};
  const int gq = lane >> 4, q = (lane & 15) >> 2, p = lane & 3, fr = lane & 15;
  auto compute = [&](int it) {
    const unsigned char* buf = ring + (it % 3) * 9216;
    bf16x8 af[4], bfr[5];
#pragma unroll
    for (int mi = 0; mi < 4; ++mi) {
      bf16x4 part[2];
#pragma unroll
      for (int t = 0; t < 2; ++t) {
        const int row = 8 * gq + 4 * t + q;
        const int ch = (2 * mi + (p >> 1)) ^ ((((row >> 1) & 1) << 1) | (((row >> 3) & 1) << 2));
        part[t] = trread(buf + row * 128 + ch * 16 + 8 * (p & 1));
      }
      af[mi] = cat8(part[0], part[1]);
    }
#pragma unroll
    for (int ni = 0; ni < 5; ++ni) {
      const int row = 16 * ni + fr;
      bfr[ni] = *(const bf16x8*)(buf + 4096 + row * 64 + 16 * (gq ^ ((row >> 1) & 3)));
    }
#pragma unroll
    for (int mi = 0; mi < 4; ++mi)
#pragma unroll
      for (int ni = 0; ni < 5; ++ni)
        acc[mi][ni] = __builtin_amdgcn_mfma_f32_16x16x32_bf16(af[mi], bfr[ni], acc[mi][ni], 0, 0, 0);
  };
  stage(0); stage(1);
  for (int it = 0; it < 10; ++it) {
    stage(it + 2);
    WAIT_V(18);
    compute(it);
  }
  WAIT_V(9); compute(10);
  WAIT_V(0); compute(11);
  __syncthreads();
  float* red = (float*)sm;
#pragma unroll
  for (int mi = 0; mi < 4; ++mi)
#pragma unroll
    for (int ni = 0; ni < 5; ++ni)
#pragma unroll
      for (int r = 0; r < 4; ++r)
        red[w * 5184 + (16 * mi + 4 * gq + r) * 81 + 16 * ni + fr] = acc[mi][ni][r];
  __syncthreads();
  {
    const int m = tid >> 2, nb = (tid & 3) * 20;
    const float bb = bf2f(b2[s0 + m]);
    const u16* hr = hbuf + (size_t)(s0 + m) * DM + d0 + nb;
    u16* orow = out + (size_t)(s0 + m) * DM + d0 + nb;
#pragma unroll
    for (int j = 0; j < 20; j += 2) {
      float s_0 = 0.f, s_1 = 0.f;
#pragma unroll
      for (int ww = 0; ww < 4; ++ww) { s_0 += red[ww * 5184 + m * 81 + nb + j]; s_1 += red[ww * 5184 + m * 81 + nb + j + 1]; }
      const unsigned hv = *(const unsigned*)(hr + j);
      const float y0 = rbf(rbf(s_0) + bb), y1 = rbf(rbf(s_1) + bb);
      const u16 o0 = f2bf(bf2f((u16)(hv & 0xffff)) + y0), o1 = f2bf(bf2f((u16)(hv >> 16)) + y1);
      *(unsigned*)(orow + j) = (unsigned)o0 | ((unsigned)o1 << 16);
    }
  }
}

extern "C" void kernel_launch(void* const* d_in, const int* in_sizes, int n_in,
                              void* d_out, int out_size, void* d_ws, size_t ws_size,
                              hipStream_t stream) {
  const u16* x     = (const u16*)d_in[0];
  const u16* logA  = (const u16*)d_in[1];
  const u16* Aim   = (const u16*)d_in[2];
  const u16* Cre   = (const u16*)d_in[3];
  const u16* Cim   = (const u16*)d_in[4];
  const u16* logdt = (const u16*)d_in[5];
  const u16* Dskip = (const u16*)d_in[6];
  const u16* w1    = (const u16*)d_in[8];
  const u16* b1    = (const u16*)d_in[9];
  const u16* w2    = (const u16*)d_in[10];
  const u16* b2    = (const u16*)d_in[11];
  unsigned char* ws = (unsigned char*)d_ws;
  float* statm = (float*)(ws);
  float* statd = (float*)(ws + 8192);
  u16* Khi = (u16*)(ws + 16384);
  u16* Klo = Khi + (size_t)DM * SL;
  u16* aT  = Klo + (size_t)DM * SL;
  u16* hb  = aT + (size_t)DM * SL;
  u16* hn  = hb + (size_t)DM * SL;
  u16* g   = hn + (size_t)DM * SL;
  u16* out = (u16*)d_out;
  prep_kernel<<<DM + 128, 256, 0, stream>>>(x, logA, Aim, Cre, Cim, logdt, statm, statd, Khi, Klo);
  conv_kernel<<<DM, 256, 0, stream>>>(x, statm, statd, Khi, Klo, Dskip, aT);
  ln2_kernel<<<SL / 16, 256, 0, stream>>>(x, aT, hb, hn);
  gemm1_kernel<<<240, 256, 0, stream>>>(hn, w1, b1, g);
  gemm2_kernel<<<256, 256, 0, stream>>>(w2, g, b2, hb, out);
}
```

```cpp
#include <hip/hip_runtime.h>
#include <hip/hip_bf16.h>
#include <stdint.h>

typedef unsigned short u16;
typedef __attribute__((ext_vector_type(8))) short bf16x8;
typedef __attribute__((ext_vector_type(4))) short bf16x4;
typedef __attribute__((ext_vector_type(4))) float f32x4;
typedef __attribute__((ext_vector_type(16))) float f32x16;
typedef __attribute__((address_space(3))) bf16x4 lds_bf16x4;

constexpr int SL = 2048;
constexpr int DM = 640;
constexpr int NM = 192;
constexpr int FF = 1536;

__device__ __forceinline__ float bf2f(u16 v) { return __uint_as_float(((unsigned)v) << 16); }
__device__ __forceinline__ u16 f2bf(float f) { __hip_bfloat16 b = __float2bfloat16(f); return __builtin_bit_cast(u16, b); }
__device__ __forceinline__ float rbf(float f) { return bf2f(f2bf(f)); }

__device__ __forceinline__ float wave_sum(float v) {
#pragma unroll
  for (int o = 32; o >= 1; o >>= 1) v += __shfl_xor(v, o, 64);
  return v;
}

constexpr int URS = 2064;
__global__ __launch_bounds__(256) void prep_kernel(
    const u16* __restrict__ x, const u16* __restrict__ logA, const u16* __restrict__ Aim,
    const u16* __restrict__ Cre, const u16* __restrict__ Cim, const u16* __restrict__ logdt,
    u16* __restrict__ urG, u16* __restrict__ Khi, u16* __restrict__ Klo) {
  __shared__ __attribute__((aligned(16))) unsigned char psm[16 * 642 * 2];
  const int tid = threadIdx.x, w = tid >> 6, lane = tid & 63;
  if (blockIdx.x >= DM) {
    u16* tile = (u16*)psm;
    const int l0 = (blockIdx.x - DM) * 16;
    float v[4][10];
#pragma unroll
    for (int rr = 0; rr < 4; ++rr) {
      const unsigned* xr = (const unsigned*)(x + (size_t)(l0 + w * 4 + rr) * DM);
#pragma unroll
      for (int k = 0; k < 5; ++k) {
        unsigned p = xr[lane + 64 * k];
        v[rr][2 * k] = bf2f((u16)(p & 0xffff)); v[rr][2 * k + 1] = bf2f((u16)(p >> 16));
      }
    }
    float s[4], q[4];
#pragma unroll
    for (int rr = 0; rr < 4; ++rr) { s[rr] = 0.f;
#pragma unroll
      for (int k = 0; k < 10; ++k) s[rr] += v[rr][k]; }
#pragma unroll
    for (int o = 32; o >= 1; o >>= 1)
#pragma unroll
      for (int rr = 0; rr < 4; ++rr) s[rr] += __shfl_xor(s[rr], o, 64);
#pragma unroll
    for (int rr = 0; rr < 4; ++rr) { const float mean = s[rr] * (1.0f / DM); q[rr] = 0.f;
#pragma unroll
      for (int k = 0; k < 10; ++k) { float dlt = v[rr][k] - mean; q[rr] += dlt * dlt; } }
#pragma unroll
    for (int o = 32; o >= 1; o >>= 1)
#pragma unroll
      for (int rr = 0; rr < 4; ++rr) q[rr] += __shfl_xor(q[rr], o, 64);
#pragma unroll
    for (int rr = 0; rr < 4; ++rr) {
      const float mb = rbf(s[rr] * (1.0f / DM));
      const float den = rbf(sqrtf(rbf(rbf(q[rr] * (1.0f / DM)) + 1e-5f)));
      unsigned* tr = (unsigned*)(tile + (w * 4 + rr) * 642);
#pragma unroll
      for (int k = 0; k < 5; ++k) {
        const u16 a = f2bf(rbf(v[rr][2 * k] - mb) / den), b = f2bf(rbf(v[rr][2 * k + 1] - mb) / den);
        tr[lane + 64 * k] = (unsigned)a | ((unsigned)b << 16);
      }
    }
    __syncthreads();
    for (int dd = tid; dd < DM; dd += 256) {
      unsigned o[8];
#pragma unroll
      for (int e = 0; e < 8; ++e)
        o[e] = (unsigned)tile[(15 - 2 * e) * 642 + dd] | ((unsigned)tile[(14 - 2 * e) * 642 + dd] << 16);
      uint4* dst = (uint4*)(urG + (size_t)dd * URS + (2048 - l0));
      dst[0] = make_uint4(o[0], o[1], o[2], o[3]);
      dst[1] = make_uint4(o[4], o[5], o[6], o[7]);
    }
    return;
  }
  float4* prm = (float4*)psm;
  float* Kq = (float*)(psm + NM * 16);
  const int d = blockIdx.x;
  if (tid < NM) {
    const float dt = rbf(expf(bf2f(logdt[d])));
    const float ar = -rbf(expf(bf2f(logA[d * NM + tid])));
    const float ai = bf2f(Aim[d * NM + tid]);
    const float cr = bf2f(Cre[d * NM + tid]) * dt;
    const float ci = bf2f(Cim[d * NM + tid]) * dt;
    prm[tid] = make_float4(ar * dt, ai * dt, 2.0f * cr, 2.0f * ci);
  }
  __syncthreads();
  {
    const int i = lane & 15, kq = lane >> 4;
    float pc, pf;
    if (w == 0) { pc = 16.0f * i; pf = (float)i; }
    else { const int step = 1 << w; pc = (float)((128 << w) + 16 * step * i); pf = (float)(step * i); }
    f32x4 accA = {0.f, 0.f, 0.f, 0.f}, accB = {0.f, 0.f, 0.f, 0.f};
    const float inv2pi = 0.15915494309189535f;
#pragma unroll 4
    for (int n0 = 0; n0 < NM; n0 += 4) {
      const float4 P = prm[n0 + kq];
      float ec = __expf(P.x * pc);
      float rc = (P.y * pc) * inv2pi; rc -= rintf(rc);
      float sc = __builtin_amdgcn_sinf(rc), cc = __builtin_amdgcn_cosf(rc);
      float gr = ec * (P.z * cc - P.w * sc);
      float gi = ec * (P.z * sc + P.w * cc);
      float ef = __expf(P.x * pf);
      float rf = (P.y * pf) * inv2pi; rf -= rintf(rf);
      float sf = __builtin_amdgcn_sinf(rf), cf = __builtin_amdgcn_cosf(rf);
      float fr = ef * cf, fi = ef * sf;
      accA = __builtin_amdgcn_mfma_f32_16x16x4f32(gr, fr, accA, 0, 0, 0);
      accB = __builtin_amdgcn_mfma_f32_16x16x4f32(-gi, fi, accB, 0, 0, 0);
    }
#pragma unroll
    for (int r = 0; r < 4; ++r) {
      const int row = 4 * kq + r;
      const float val = accA[r] + accB[r];
      if (w == 0) Kq[16 * row + i] = val;
      else if (row < 8) Kq[256 + 128 * (w - 1) + 16 * row + i] = val;
      else if (w == 3 && row == 8 && i == 0) Kq[640] = val;
    }
  }
  __syncthreads();
#pragma unroll
  for (int k = 0; k < 8; ++k) {
    const int l = tid + 256 * k;
    const int p = (int)rbf((float)l);
    const int e = 31 - __clz(p | 128);
    const int sh = e - 7;
    const int idx = (sh == 0) ? p : (128 * (sh + 1) + ((p - (128 << sh)) >> sh));
    const float v = Kq[idx];
    const u16 hi = f2bf(v);
    const u16 lo = f2bf(v - bf2f(hi));
    Khi[(size_t)d * SL + l] = hi;
    Klo[(size_t)d * SL + l] = lo;
  }
}

constexpr int CV_UC = 0;
constexpr int CV_KH = 8 * 4352;
constexpr int CV_KL = CV_KH + 7680;
constexpr int CV_UR = CV_KL + 7680;
constexpr int CV_TOTAL = CV_UR + 4224;
__device__ __forceinline__ int copy_off(int t) { return (t < 2) ? t : (5 + 4 * ((t >= 5) ? (t - 5) : (t - 2))); }

__global__ __launch_bounds__(256) void conv_kernel(
    const u16* __restrict__ urG, const u16* __restrict__ Khi, const u16* __restrict__ Klo,
    const u16* __restrict__ Dskip, u16* __restrict__ aT) {
  __shared__ __attribute__((aligned(16))) unsigned char sm[CV_TOTAL];
  const int tid = threadIdx.x, w = tid >> 6, lane = tid & 63;
  const int d = blockIdx.x;
  {
    const uint4 v0 = *(const uint4*)(urG + (size_t)d * URS + tid * 8);
    const uint4 vh = *(const uint4*)(Khi + (size_t)d * SL + tid * 8);
    const uint4 vl = *(const uint4*)(Klo + (size_t)d * SL + tid * 8);
    *(uint4*)(sm + CV_UR + tid * 16) = v0;
    if (tid < 2) *(uint4*)(sm + CV_UR + (256 + tid) * 16) = *(const uint4*)(urG + (size_t)d * URS + (256 + tid) * 8);
    else if (tid < 7) *(uint4*)(sm + CV_UR + (256 + tid) * 16) = make_uint4(0, 0, 0, 0);
    const int b = tid >> 2, part = tid & 3;
    *(uint4*)(sm + CV_KH + (b + 32) * 80 + part * 16) = vh;
    *(uint4*)(sm + CV_KL + (b + 32) * 80 + part * 16) = vl;
    if (tid < 160) {
      *(uint4*)(sm + CV_KH + tid * 16) = make_uint4(0, 0, 0, 0);
      *(uint4*)(sm + CV_KL + tid * 16) = make_uint4(0, 0, 0, 0);
    }
  }
  __syncthreads();
  for (int q = tid; q < 260; q += 256) {
    const uint4 A = *(const uint4*)(sm + CV_UR + (q + 1) * 16);
    const uint4 B = *(const uint4*)(sm + CV_UR + (q + 2) * 16);
    const uint4 Cc = *(const uint4*)(sm + CV_UR + (q + 3) * 16);
    const unsigned dw[12] = {A.x, A.y, A.z, A.w, B.x, B.y, B.z, B.w, Cc.x, Cc.y, Cc.z, Cc.w};
#pragma unroll
    for (int t = 0; t < 8; ++t) {
      const int o = 7 + t;
      uint4 r;
      if (o & 1) {
        const int k = o >> 1;
        r.x = __builtin_amdgcn_alignbit(dw[k + 1], dw[k], 16); r.y = __builtin_amdgcn_alignbit(dw[k + 2], dw[k + 1], 16);
        r.z = __builtin_amdgcn_alignbit(dw[k + 3], dw[k + 2], 16); r.w = __builtin_amdgcn_alignbit(dw[k + 4], dw[k + 3], 16);
      } else {
        const int k = o >> 1;
        r.x = dw[k]; r.y = dw[k + 1]; r.z = dw[k + 2]; r.w = dw[k + 3];
      }
      *(uint4*)(sm + CV_UC + t * 4352 + 16 * (copy_off(t) + q)) = r;
    }
  }
  __syncthreads();
  const int r = lane & 31, h = lane >> 5;
  const int t = (8 - (r & 7)) & 7;
  const int aBase = CV_UC + t * 4352 + 16 * (copy_off(t) - (r >> 3) - ((r & 7) ? 1 : 0) + h);
  f32x16 acc0 = {}, acc1 = {};
  bf16x8 af[2][2], bh1[2][2], bl1[2][2], bh0[2][2], bl0[2][2];
  auto loadf = [&](int i, int buf) {
    const int dl = w + 4 * i;
#pragma unroll
    for (int s = 0; s < 2; ++s) {
      af[buf][s] = *(const bf16x8*)(sm + aBase + 16 * (256 - 4 * dl + 2 * s));
      const int kb1 = (64 + r - dl) * 80 + 32 * s + 16 * h;
      bh1[buf][s] = *(const bf16x8*)(sm + CV_KH + kb1);
      bl1[buf][s] = *(const bf16x8*)(sm + CV_KL + kb1);
      if (i < 8) {
        bh0[buf][s] = *(const bf16x8*)(sm + CV_KH + kb1 - 32 * 80);
        bl0[buf][s] = *(const bf16x8*)(sm + CV_KL + kb1 - 32 * 80);
      }
    }
  };
  loadf(0, 0);
#pragma unroll
  for (int i = 0; i < 16; ++i) {
    if (i + 1 < 16) loadf(i + 1, (i + 1) & 1);
    const int b = i & 1;
#pragma unroll
    for (int s = 0; s < 2; ++s) {
      acc1 = __builtin_amdgcn_mfma_f32_32x32x16_bf16(af[b][s], bh1[b][s], acc1, 0, 0, 0);
      acc1 = __builtin_amdgcn_mfma_f32_32x32x16_bf16(af[b][s], bl1[b][s], acc1, 0, 0, 0);
      if (i < 8) {
        acc0 = __builtin_amdgcn_mfma_f32_32x32x16_bf16(af[b][s], bh0[b][s], acc0, 0, 0, 0);
        acc0 = __builtin_amdgcn_mfma_f32_32x32x16_bf16(af[b][s], bl0[b][s], acc0, 0, 0, 0);
      }
    }
  }
  __syncthreads();
  float* red = (float*)(sm + CV_UC);
#pragma unroll
  for (int i = 0; i < 16; ++i) {
    const int row = (i & 3) + 8 * (i >> 2) + 4 * h;
    red[(w * 2 + 0) * 1056 + r * 33 + row] = acc0[i];
    red[(w * 2 + 1) * 1056 + r * 33 + row] = acc1[i];
  }
  __syncthreads();
  const float dsk = bf2f(Dskip[d]);
  const u16* urP = (const u16*)(sm + CV_UR);
#pragma unroll
  for (int k = 0; k < 4; ++k) {
    const int l = 2 * (tid + 256 * k);
    float y[2];
#pragma unroll
    for (int e = 0; e < 2; ++e) {
      const int ll = l + e, a = ll >> 5, rr = ll & 31;
      const int tile = a >> 5, col = a & 31;
      float acc = 0.f;
#pragma unroll
      for (int ww = 0; ww < 4; ++ww) acc += red[(ww * 2 + tile) * 1056 + col * 33 + rr];
      const float uv = bf2f(urP[2063 - ll]);
      y[e] = rbf(acc) + rbf(uv * dsk);
    }
    *(unsigned*)(aT + (size_t)d * SL + l) = (unsigned)f2bf(y[0]) | ((unsigned)f2bf(y[1]) << 16);
  }
}

__global__ __launch_bounds__(256) void ln2_kernel(
    const u16* __restrict__ x, const u16* __restrict__ aT, u16* __restrict__ hbuf, u16* __restrict__ hn) {
  __shared__ __attribute__((aligned(16))) u16 tile[DM * 10];
  const int tid = threadIdx.x, w = tid >> 6, lane = tid & 63;
  const int l0 = blockIdx.x * 8;
  unsigned xv[2][5];
#pragma unroll
  for (int rr = 0; rr < 2; ++rr) {
    const unsigned* xr = (const unsigned*)(x + (size_t)(l0 + w * 2 + rr) * DM);
#pragma unroll
    for (int k = 0; k < 5; ++k) xv[rr][k] = xr[lane + 64 * k];
  }
  for (int dd = tid; dd < DM; dd += 256) {
    const uint4 v = *(const uint4*)(aT + (size_t)dd * SL + l0);
    unsigned* dst = (unsigned*)(tile + dd * 10);
    dst[0] = v.x; dst[1] = v.y; dst[2] = v.z; dst[3] = v.w;
  }
  __syncthreads();
  float v[2][10];
#pragma unroll
  for (int rr = 0; rr < 2; ++rr) {
    const int rl = w * 2 + rr;
#pragma unroll
    for (int k = 0; k < 5; ++k) {
      const unsigned p = xv[rr][k];
      const int d0 = 2 * (lane + 64 * k);
      v[rr][2 * k] = rbf(bf2f((u16)(p & 0xffff)) + bf2f(tile[d0 * 10 + rl]));
      v[rr][2 * k + 1] = rbf(bf2f((u16)(p >> 16)) + bf2f(tile[(d0 + 1) * 10 + rl]));
    }
    unsigned* hr = (unsigned*)(hbuf + (size_t)(l0 + rl) * DM);
#pragma unroll
    for (int k = 0; k < 5; ++k) hr[lane + 64 * k] = (unsigned)f2bf(v[rr][2 * k]) | ((unsigned)f2bf(v[rr][2 * k + 1]) << 16);
  }
#pragma unroll
  for (int pass = 0; pass < 2; ++pass) {
    float s[2], q[2];
#pragma unroll
    for (int rr = 0; rr < 2; ++rr) { s[rr] = 0.f;
#pragma unroll
      for (int k = 0; k < 10; ++k) s[rr] += v[rr][k]; }
#pragma unroll
    for (int o = 32; o >= 1; o >>= 1) { s[0] += __shfl_xor(s[0], o, 64); s[1] += __shfl_xor(s[1], o, 64); }
#pragma unroll
    for (int rr = 0; rr < 2; ++rr) { const float mean = s[rr] * (1.0f / DM); q[rr] = 0.f;
#pragma unroll
      for (int k = 0; k < 10; ++k) { float dlt = v[rr][k] - mean; q[rr] += dlt * dlt; } }
#pragma unroll
    for (int o = 32; o >= 1; o >>= 1) { q[0] += __shfl_xor(q[0], o, 64); q[1] += __shfl_xor(q[1], o, 64); }
#pragma unroll
    for (int rr = 0; rr < 2; ++rr) {
      const float mb = rbf(s[rr] * (1.0f / DM));
      const float den = rbf(sqrtf(rbf(rbf(q[rr] * (1.0f / DM)) + 1e-5f)));
#pragma unroll
      for (int k = 0; k < 10; ++k) v[rr][k] = rbf(rbf(v[rr][k] - mb) / den);
    }
  }
#pragma unroll
  for (int rr = 0; rr < 2; ++rr) {
    unsigned* nr = (unsigned*)(hn + (size_t)(l0 + w * 2 + rr) * DM);
#pragma unroll
    for (int k = 0; k < 5; ++k) nr[lane + 64 * k] = (unsigned)f2bf(v[rr][2 * k]) | ((unsigned)f2bf(v[rr][2 * k + 1]) << 16);
  }
}

#define WAIT_V(n) asm volatile("s_waitcnt vmcnt(" #n ")" ::: "memory")
__device__ __forceinline__ void glds16(const void* g, unsigned lds_dst) {
  unsigned keep;
  asm volatile("s_mov_b32 %0, m0\n\ts_mov_b32 m0, %2\n\ts_nop 0\n\tglobal_load_lds_dwordx4 %1, off\n\ts_mov_b32 m0, %0"
               : "=&s"(keep) : "v"(g), "s"(lds_dst) : "memory");
}
__device__ __forceinline__ unsigned lds_addr(const void* p) { return (unsigned)(size_t)p; }
__device__ __forceinline__ bf16x4 trread(const unsigned char* p) {
  return __builtin_amdgcn_ds_read_tr16_b64_v4i16((lds_bf16x4*)p);
}
__device__ __forceinline__ bf16x8 cat8(bf16x4 a, bf16x4 b) {
  bf16x8 r; r[0] = a[0]; r[1] = a[1]; r[2] = a[2]; r[3] = a[3]; r[4] = b[0]; r[5] = b[1]; r[6] = b[2]; r[7] = b[3]; return r;
}
__device__ __forceinline__ float gelu_tanh(float v) {
  const float k0 = 0.7978845608028654f, k1 = 0.044715f;
  float inner = k0 * (v + k1 * v * v * v);
  return 0.5f * v * (1.0f + tanhf(inner));
}

__global__ __launch_bounds__(256, 1) void gemm1_kernel(
    const u16* __restrict__ hn, const u16* __restrict__ w1, const u16* __restrict__ b1, u16* __restrict__ g) {
  __shared__ __attribute__((aligned(16))) unsigned char sm[4 * 4 * 8192];
  const int tid = threadIdx.x, w = tid >> 6, lane = tid & 63;
  const int xg = blockIdx.x & 7, li = blockIdx.x >> 3;
  const int nt = xg * 3 + li / 10, mt = li % 10;
  const int m0 = mt * 64, n0 = nt * 64;
  unsigned char* ring = sm + w * 4 * 8192;
  const unsigned ringa = __builtin_amdgcn_readfirstlane(lds_addr(ring));
  const int kbase = w * 512;
  const int srow = lane >> 3, sch = lane & 7;
  auto stage = [&](int it) {
    const unsigned dst = ringa + (it & 3) * 8192;
    const int k0 = kbase + it * 32;
#pragma unroll
    for (int i = 0; i < 4; ++i) {
      const int row = 8 * i + srow;
      const int ch = sch ^ (((row >> 1) & 1) << 2);
      glds16(hn + (size_t)(k0 + row) * DM + m0 + ch * 8, dst + i * 1024);
    }
#pragma unroll
    for (int i = 0; i < 4; ++i) {
      const int row = 8 * i + srow;
      const int ch = sch ^ (((row >> 1) & 1) << 2);
      glds16(w1 + (size_t)(k0 + row) * FF + n0 + ch * 8, dst + 4096 + i * 1024);
    }
  };
  f32x16 acc[2][2] = {};
  const int gq = lane >> 4, q = (lane & 15) >> 2, p = lane & 3;
  auto compute = [&](int it) {
    const unsigned char* buf = ring + (it & 3) * 8192;
#pragma unroll
    for (int ks = 0; ks < 2; ++ks) {
      bf16x8 af[2], bfr[2];
#pragma unroll
      for (int mi = 0; mi < 2; ++mi) {
        bf16x4 part[2];
#pragma unroll
        for (int t = 0; t < 2; ++t) {
          const int row = 16 * ks + 8 * (gq >> 1) + 4 * t + q;
          const int ch = (4 * mi + 2 * (gq & 1) + (p >> 1)) ^ (((row >> 1) & 1) << 2);
          part[t] = trread(buf + row * 128 + ch * 16 + 8 * (p & 1));
        }
        af[mi] = cat8(part[0], part[1]);
#pragma unroll
        for (int t = 0; t < 2; ++t) {
          const int row = 16 * ks + 8 * (gq >> 1) + 4 * t + q;
          const int ch = (4 * mi + 2 * (gq & 1) + (p >> 1)) ^ (((row >> 1) & 1) << 2);
          part[t] = trread(buf + 4096 + row * 128 + ch * 16 + 8 * (p & 1));
        }
        bfr[mi] = cat8(part[0], part[1]);
      }
#pragma unroll
      for (int mi = 0; mi < 2; ++mi)
#pragma unroll
        for (int ni = 0; ni < 2; ++ni)
          acc[mi][ni] = __builtin_amdgcn_mfma_f32_32x32x16_bf16(af[mi], bfr[ni], acc[mi][ni], 0, 0, 0);
    }
  };
  stage(0); stage(1); stage(2);
  for (int it = 0; it < 13; ++it) {
    stage(it + 3);
    WAIT_V(24);
    compute(it);
  }
  WAIT_V(16); compute(13);
  WAIT_V(8); compute(14);
  WAIT_V(0); compute(15);
  __syncthreads();
  float* red = (float*)sm;
  {
    const int h = lane >> 5, c = lane & 31;
#pragma unroll
    for (int mi = 0; mi < 2; ++mi)
#pragma unroll
      for (int ni = 0; ni < 2; ++ni)
#pragma unroll
        for (int i = 0; i < 16; ++i) {
          const int m = 32 * mi + (i & 3) + 8 * (i >> 2) + 4 * h;
          red[w * 4160 + m * 65 + 32 * ni + c] = acc[mi][ni][i];
        }
  }
  __syncthreads();
  {
    const int m = tid >> 2, nb = (tid & 3) * 16;
    u16 o[16];
#pragma unroll
    for (int j = 0; j < 16; ++j) {
      float s = 0.f;
#pragma unroll
      for (int ww = 0; ww < 4; ++ww) s += red[ww * 4160 + m * 65 + nb + j];
      const float v = rbf(rbf(s) + bf2f(b1[n0 + nb + j]));
      o[j] = f2bf(gelu_tanh(v));
    }
    uint4* dst = (uint4*)(g + (size_t)(m0 + m) * FF + n0 + nb);
    uint4 v0, v1;
    v0.x = o[0] | ((unsigned)o[1] << 16); v0.y = o[2] | ((unsigned)o[3] << 16); v0.z = o[4] | ((unsigned)o[5] << 16); v0.w = o[6] | ((unsigned)o[7] << 16);
    v1.x = o[8] | ((unsigned)o[9] << 16); v1.y = o[10] | ((unsigned)o[11] << 16); v1.z = o[12] | ((unsigned)o[13] << 16); v1.w = o[14] | ((unsigned)o[15] << 16);
    dst[0] = v0; dst[1] = v1;
  }
}

__global__ __launch_bounds__(256, 1) void gemm2_kernel(
    const u16* __restrict__ w2, const u16* __restrict__ g, const u16* __restrict__ b2,
    const u16* __restrict__ hbuf, u16* __restrict__ out) {
  __shared__ __attribute__((aligned(16))) unsigned char sm[4 * 4 * 9216];
  const int tid = threadIdx.x, w = tid >> 6, lane = tid & 63;
  const int xg = blockIdx.x & 7, li = blockIdx.x >> 3;
  const int st = xg * 4 + (li >> 3), dtile = li & 7;
  const int s0 = st * 64, d0 = dtile * 80;
  unsigned char* ring = sm + w * 4 * 9216;
  const unsigned ringa = __builtin_amdgcn_readfirstlane(lds_addr(ring));
  const int kbase = w * 384;
  auto stage = [&](int it) {
    const unsigned dst = ringa + (it & 3) * 9216;
    const int k0 = kbase + it * 32;
#pragma unroll
    for (int i = 0; i < 4; ++i) {
      const int row = 8 * i + (lane >> 3);
      const int ch = (lane & 7) ^ ((((row >> 1) & 1) << 1) | (((row >> 3) & 1) << 2));
      glds16(w2 + (size_t)(k0 + row) * SL + s0 + ch * 8, dst + i * 1024);
    }
#pragma unroll
    for (int i = 0; i < 5; ++i) {
      const int row = 16 * i + (lane >> 2);
      const int ch = (lane & 3) ^ ((row >> 1) & 3);
      glds16(g + (size_t)(d0 + row) * FF + k0 + ch * 8, dst + 4096 + i * 1024);
    }
  };
  f32x4 acc[4][5] = {};
  const int gq = lane >> 4, q = (lane & 15) >> 2, p = lane & 3, fr = lane & 15;
  auto compute = [&](int it) {
    const unsigned char* buf = ring + (it & 3) * 9216;
    bf16x8 af[4], bfr[5];
#pragma unroll
    for (int mi = 0; mi < 4; ++mi) {
      bf16x4 part[2];
#pragma unroll
      for (int t = 0; t < 2; ++t) {
        const int row = 8 * gq + 4 * t + q;
        const int ch = (2 * mi + (p >> 1)) ^ ((((row >> 1) & 1) << 1) | (((row >> 3) & 1) << 2));
        part[t] = trread(buf + row * 128 + ch * 16 + 8 * (p & 1));
      }
      af[mi] = cat8(part[0], part[1]);
    }
#pragma unroll
    for (int ni = 0; ni < 5; ++ni) {
      const int row = 16 * ni + fr;
      bfr[ni] = *(const bf16x8*)(buf + 4096 + row * 64 + 16 * (gq ^ ((row >> 1) & 3)));
    }
#pragma unroll
    for (int mi = 0; mi < 4; ++mi)
#pragma unroll
      for (int ni = 0; ni < 5; ++ni)
        acc[mi][ni] = __builtin_amdgcn_mfma_f32_16x16x32_bf16(af[mi], bfr[ni], acc[mi][ni], 0, 0, 0);
  };
  stage(0); stage(1); stage(2);
  for (int it = 0; it < 9; ++it) {
    stage(it + 3);
    WAIT_V(27);
    compute(it);
  }
  WAIT_V(18); compute(9);
  WAIT_V(9); compute(10);
  WAIT_V(0); compute(11);
  __syncthreads();
  float* red = (float*)sm;
#pragma unroll
  for (int mi = 0; mi < 4; ++mi)
#pragma unroll
    for (int ni = 0; ni < 5; ++ni)
#pragma unroll
      for (int r = 0; r < 4; ++r)
        red[w * 5184 + (16 * mi + 4 * gq + r) * 81 + 16 * ni + fr] = acc[mi][ni][r];
  __syncthreads();
  {
    const int m = tid >> 2, nb = (tid & 3) * 20;
    const float bb = bf2f(b2[s0 + m]);
    const u16* hr = hbuf + (size_t)(s0 + m) * DM + d0 + nb;
    u16* orow = out + (size_t)(s0 + m) * DM + d0 + nb;
#pragma unroll
    for (int j = 0; j < 20; j += 2) {
      float s_0 = 0.f, s_1 = 0.f;
#pragma unroll
      for (int ww = 0; ww < 4; ++ww) { s_0 += red[ww * 5184 + m * 81 + nb + j]; s_1 += red[ww * 5184 + m * 81 + nb + j + 1]; }
      const unsigned hv = *(const unsigned*)(hr + j);
      const float y0 = rbf(rbf(s_0) + bb), y1 = rbf(rbf(s_1) + bb);
      const u16 o0 = f2bf(bf2f((u16)(hv & 0xffff)) + y0), o1 = f2bf(bf2f((u16)(hv >> 16)) + y1);
      *(unsigned*)(orow + j) = (unsigned)o0 | ((unsigned)o1 << 16);
    }
  }
}

extern "C" void kernel_launch(void* const* d_in, const int* in_sizes, int n_in,
                              void* d_out, int out_size, void* d_ws, size_t ws_size,
                              hipStream_t stream) {
  const u16* x     = (const u16*)d_in[0];
  const u16* logA  = (const u16*)d_in[1];
  const u16* Aim   = (const u16*)d_in[2];
  const u16* Cre   = (const u16*)d_in[3];
  const u16* Cim   = (const u16*)d_in[4];
  const u16* logdt = (const u16*)d_in[5];
  const u16* Dskip = (const u16*)d_in[6];
  const u16* w1    = (const u16*)d_in[8];
  const u16* b1    = (const u16*)d_in[9];
  const u16* w2    = (const u16*)d_in[10];
  const u16* b2    = (const u16*)d_in[11];
  unsigned char* ws = (unsigned char*)d_ws;
  u16* Khi = (u16*)(ws + 16384);
  u16* Klo = Khi + (size_t)DM * SL;
  u16* aT  = Klo + (size_t)DM * SL;
  u16* hb  = aT + (size_t)DM * SL;
  u16* hn  = hb + (size_t)DM * SL;
  u16* g   = hn + (size_t)DM * SL;
  u16* urG = g + (size_t)DM * FF;
  u16* out = (u16*)d_out;
  prep_kernel<<<DM + 128, 256, 0, stream>>>(x, logA, Aim, Cre, Cim, logdt, urG, Khi, Klo);
  conv_kernel<<<DM, 256, 0, stream>>>(urG, Khi, Klo, Dskip, aT);
  ln2_kernel<<<SL / 8, 256, 0, stream>>>(x, aT, hb, hn);
  gemm1_kernel<<<240, 256, 0, stream>>>(hn, w1, b1, g);
  gemm2_kernel<<<256, 256, 0, stream>>>(w2, g, b2, hb, out);
}
```

```cpp
#include <hip/hip_runtime.h>
#include <hip/hip_bf16.h>
#include <stdint.h>

typedef unsigned short u16;
typedef __attribute__((ext_vector_type(8))) short bf16x8;
typedef __attribute__((ext_vector_type(4))) short bf16x4;
typedef __attribute__((ext_vector_type(4))) float f32x4;
typedef __attribute__((ext_vector_type(16))) float f32x16;
typedef __attribute__((address_space(3))) bf16x4 lds_bf16x4;

constexpr int SL = 2048;
constexpr int DM = 640;
constexpr int NM = 192;
constexpr int FF = 1536;

__device__ __forceinline__ float bf2f(u16 v) { return __uint_as_float(((unsigned)v) << 16); }
__device__ __forceinline__ u16 f2bf(float f) { __hip_bfloat16 b = __float2bfloat16(f); return __builtin_bit_cast(u16, b); }
__device__ __forceinline__ float rbf(float f) { return bf2f(f2bf(f)); }

__device__ __forceinline__ float wave_sum(float v) {
#pragma unroll
  for (int o = 32; o >= 1; o >>= 1) v += __shfl_xor(v, o, 64);
  return v;
}

constexpr int URS = 2064;
__global__ __launch_bounds__(256) void prep_kernel(
    const u16* __restrict__ x, const u16* __restrict__ logA, const u16* __restrict__ Aim,
    const u16* __restrict__ Cre, const u16* __restrict__ Cim, const u16* __restrict__ logdt,
    u16* __restrict__ urG, u16* __restrict__ Khi, u16* __restrict__ Klo) {
  __shared__ __attribute__((aligned(16))) unsigned char psm[16 * 642 * 2];
  const int tid = threadIdx.x, w = tid >> 6, lane = tid & 63;
  if (blockIdx.x < 128) {
    u16* tile = (u16*)psm;
    const int l0 = blockIdx.x * 16;
    float v[4][10];
#pragma unroll
    for (int rr = 0; rr < 4; ++rr) {
      const unsigned* xr = (const unsigned*)(x + (size_t)(l0 + w * 4 + rr) * DM);
#pragma unroll
      for (int k = 0; k < 5; ++k) {
        unsigned p = xr[lane + 64 * k];
        v[rr][2 * k] = bf2f((u16)(p & 0xffff)); v[rr][2 * k + 1] = bf2f((u16)(p >> 16));
      }
    }
    float s[4], q[4];
#pragma unroll
    for (int rr = 0; rr < 4; ++rr) { s[rr] = 0.f;
#pragma unroll
      for (int k = 0; k < 10; ++k) s[rr] += v[rr][k]; }
#pragma unroll
    for (int o = 32; o >= 1; o >>= 1)
#pragma unroll
      for (int rr = 0; rr < 4; ++rr) s[rr] += __shfl_xor(s[rr], o, 64);
#pragma unroll
    for (int rr = 0; rr < 4; ++rr) { const float mean = s[rr] * (1.0f / DM); q[rr] = 0.f;
#pragma unroll
      for (int k = 0; k < 10; ++k) { float dlt = v[rr][k] - mean; q[rr] += dlt * dlt; } }
#pragma unroll
    for (int o = 32; o >= 1; o >>= 1)
#pragma unroll
      for (int rr = 0; rr < 4; ++rr) q[rr] += __shfl_xor(q[rr], o, 64);
#pragma unroll
    for (int rr = 0; rr < 4; ++rr) {
      const float mb = rbf(s[rr] * (1.0f / DM));
      const float den = rbf(sqrtf(rbf(rbf(q[rr] * (1.0f / DM)) + 1e-5f)));
      unsigned* tr = (unsigned*)(tile + (w * 4 + rr) * 642);
#pragma unroll
      for (int k = 0; k < 5; ++k) {
        const u16 a = f2bf(rbf(v[rr][2 * k] - mb) / den), b = f2bf(rbf(v[rr][2 * k + 1] - mb) / den);
        tr[lane + 64 * k] = (unsigned)a | ((unsigned)b << 16);
      }
    }
    __syncthreads();
    for (int dd = tid; dd < DM; dd += 256) {
      unsigned o[8];
#pragma unroll
      for (int e = 0; e < 8; ++e)
        o[e] = (unsigned)tile[(15 - 2 * e) * 642 + dd] | ((unsigned)tile[(14 - 2 * e) * 642 + dd] << 16);
      uint4* dst = (uint4*)(urG + (size_t)dd * URS + (2048 - l0));
      dst[0] = make_uint4(o[0], o[1], o[2], o[3]);
      dst[1] = make_uint4(o[4], o[5], o[6], o[7]);
    }
    return;
  }
  float4* prm = (float4*)psm;
  float* Kq = (float*)(psm + NM * 16);
  const int d = blockIdx.x - 128;
  if (tid < NM) {
    const float dt = rbf(expf(bf2f(logdt[d])));
    const float ar = -rbf(expf(bf2f(logA[d * NM + tid])));
    const float ai = bf2f(Aim[d * NM + tid]);
    const float cr = bf2f(Cre[d * NM + tid]) * dt;
    const float ci = bf2f(Cim[d * NM + tid]) * dt;
    prm[tid] = make_float4(ar * dt, ai * dt, 2.0f * cr, 2.0f * ci);
  }
  __syncthreads();
  {
    const int i = lane & 15, kq = lane >> 4;
    f32x4 acc[4] = {};
    const float inv2pi = 0.15915494309189535f;
    const float pf = (float)i;
#pragma unroll 2
    for (int g = 0; g < 12; ++g) {
      const float4 P = prm[48 * w + 4 * g + kq];
      const float zi = P.y * inv2pi;
      float ef = __expf(P.x * pf);
      float rf = zi * pf; rf -= rintf(rf);
      float fr = ef * __builtin_amdgcn_cosf(rf), fi = ef * __builtin_amdgcn_sinf(rf);
#pragma unroll
      for (int sg = 0; sg < 4; ++sg) {
        const float pc = (sg == 0) ? 16.0f * i : (float)((128 << sg) + (16 << sg) * i);
        float ec = __expf(P.x * pc);
        float rc = zi * pc; rc -= rintf(rc);
        float sc = __builtin_amdgcn_sinf(rc), cc = __builtin_amdgcn_cosf(rc);
        float gr = ec * (P.z * cc - P.w * sc);
        float gi = ec * (P.z * sc + P.w * cc);
        acc[sg] = __builtin_amdgcn_mfma_f32_16x16x4f32(gr, fr, acc[sg], 0, 0, 0);
        acc[sg] = __builtin_amdgcn_mfma_f32_16x16x4f32(-gi, fi, acc[sg], 0, 0, 0);
        const float nr = fr * fr - fi * fi, ni = 2.0f * fr * fi; fr = nr; fi = ni;
      }
    }
    float* Kp = Kq + w * 648;
#pragma unroll
    for (int sg = 0; sg < 4; ++sg)
#pragma unroll
      for (int r = 0; r < 4; ++r) {
        const int row = 4 * kq + r;
        if (sg == 0) Kp[16 * row + i] = acc[sg][r];
        else if (row < 8) Kp[256 + 128 * (sg - 1) + 16 * row + i] = acc[sg][r];
        else if (sg == 3 && row == 8 && i == 0) Kp[640] = acc[sg][r];
      }
  }
  __syncthreads();
#pragma unroll
  for (int k = 0; k < 8; ++k) {
    const int l = tid + 256 * k;
    const int p = (int)rbf((float)l);
    const int e = 31 - __clz(p | 128);
    const int sh = e - 7;
    const int idx = (sh == 0) ? p : (128 * (sh + 1) + ((p - (128 << sh)) >> sh));
    const float v = (Kq[idx] + Kq[648 + idx]) + (Kq[1296 + idx] + Kq[1944 + idx]);
    const u16 hi = f2bf(v);
    const u16 lo = f2bf(v - bf2f(hi));
    Khi[(size_t)d * SL + l] = hi;
    Klo[(size_t)d * SL + l] = lo;
  }
}

constexpr int CV_UC = 0;
constexpr int CV_KH = 8 * 4352;
constexpr int CV_KL = CV_KH + 7680;
constexpr int CV_UR = CV_KL + 7680;
constexpr int CV_TOTAL = CV_UR + 4224;
__device__ __forceinline__ int copy_off(int t) { return (t < 2) ? t : (5 + 4 * ((t >= 5) ? (t - 5) : (t - 2))); }

__global__ __launch_bounds__(256) void conv_kernel(
    const u16* __restrict__ urG, const u16* __restrict__ Khi, const u16* __restrict__ Klo,
    const u16* __restrict__ Dskip, u16* __restrict__ aT) {
  __shared__ __attribute__((aligned(16))) unsigned char sm[CV_TOTAL];
  const int tid = threadIdx.x, w = tid >> 6, lane = tid & 63;
  const int d = blockIdx.x;
  {
    const uint4 v0 = *(const uint4*)(urG + (size_t)d * URS + tid * 8);
    const uint4 vh = *(const uint4*)(Khi + (size_t)d * SL + tid * 8);
    const uint4 vl = *(const uint4*)(Klo + (size_t)d * SL + tid * 8);
    *(uint4*)(sm + CV_UR + tid * 16) = v0;
    if (tid < 2) *(uint4*)(sm + CV_UR + (256 + tid) * 16) = *(const uint4*)(urG + (size_t)d * URS + (256 + tid) * 8);
    else if (tid < 7) *(uint4*)(sm + CV_UR + (256 + tid) * 16) = make_uint4(0, 0, 0, 0);
    const int b = tid >> 2, part = tid & 3;
    *(uint4*)(sm + CV_KH + (b + 32) * 80 + part * 16) = vh;
    *(uint4*)(sm + CV_KL + (b + 32) * 80 + part * 16) = vl;
    if (tid < 160) {
      *(uint4*)(sm + CV_KH + tid * 16) = make_uint4(0, 0, 0, 0);
      *(uint4*)(sm + CV_KL + tid * 16) = make_uint4(0, 0, 0, 0);
    }
  }
  __syncthreads();
  for (int q = tid; q < 260; q += 256) {
    const uint4 A = *(const uint4*)(sm + CV_UR + (q + 1) * 16);
    const uint4 B = *(const uint4*)(sm + CV_UR + (q + 2) * 16);
    const uint4 Cc = *(const uint4*)(sm + CV_UR + (q + 3) * 16);
    const unsigned dw[12] = {A.x, A.y, A.z, A.w, B.x, B.y, B.z, B.w, Cc.x, Cc.y, Cc.z, Cc.w};
#pragma unroll
    for (int t = 0; t < 8; ++t) {
      const int o = 7 + t;
      uint4 r;
      if (o & 1) {
        const int k = o >> 1;
        r.x = __builtin_amdgcn_alignbit(dw[k + 1], dw[k], 16); r.y = __builtin_amdgcn_alignbit(dw[k + 2], dw[k + 1], 16);
        r.z = __builtin_amdgcn_alignbit(dw[k + 3], dw[k + 2], 16); r.w = __builtin_amdgcn_alignbit(dw[k + 4], dw[k + 3], 16);
      } else {
        const int k = o >> 1;
        r.x = dw[k]; r.y = dw[k + 1]; r.z = dw[k + 2]; r.w = dw[k + 3];
      }
      *(uint4*)(sm + CV_UC + t * 4352 + 16 * (copy_off(t) + q)) = r;
    }
  }
  __syncthreads();
  const int r = lane & 31, h = lane >> 5;
  const int t = (8 - (r & 7)) & 7;
  const int aBase = CV_UC + t * 4352 + 16 * (copy_off(t) - (r >> 3) - ((r & 7) ? 1 : 0) + h);
  f32x16 acc0 = {}, acc1 = {};
  bf16x8 af[2][2], bh1[2][2], bl1[2][2], bh0[2][2], bl0[2][2];
  auto loadf = [&](int i, int buf) {
    const int dl = w + 4 * i;
#pragma unroll
    for (int s = 0; s < 2; ++s) {
      af[buf][s] = *(const bf16x8*)(sm + aBase + 16 * (256 - 4 * dl + 2 * s));
      const int kb1 = (64 + r - dl) * 80 + 32 * s + 16 * h;
      bh1[buf][s] = *(const bf16x8*)(sm + CV_KH + kb1);
      bl1[buf][s] = *(const bf16x8*)(sm + CV_KL + kb1);
      if (i < 8) {
        bh0[buf][s] = *(const bf16x8*)(sm + CV_KH + kb1 - 32 * 80);
        bl0[buf][s] = *(const bf16x8*)(sm + CV_KL + kb1 - 32 * 80);
      }
    }
  };
  loadf(0, 0);
#pragma unroll
  for (int i = 0; i < 16; ++i) {
    if (i + 1 < 16) loadf(i + 1, (i + 1) & 1);
    const int b = i & 1;
#pragma unroll
    for (int s = 0; s < 2; ++s) {
      acc1 = __builtin_amdgcn_mfma_f32_32x32x16_bf16(af[b][s], bh1[b][s], acc1, 0, 0, 0);
      acc1 = __builtin_amdgcn_mfma_f32_32x32x16_bf16(af[b][s], bl1[b][s], acc1, 0, 0, 0);
      if (i < 8) {
        acc0 = __builtin_amdgcn_mfma_f32_32x32x16_bf16(af[b][s], bh0[b][s], acc0, 0, 0, 0);
        acc0 = __builtin_amdgcn_mfma_f32_32x32x16_bf16(af[b][s], bl0[b][s], acc0, 0, 0, 0);
      }
    }
    if (i < 7) { __builtin_amdgcn_sched_group_barrier(0x100, 10, 0); __builtin_amdgcn_sched_group_barrier(0x8, 8, 0); }
    else if (i == 7) { __builtin_amdgcn_sched_group_barrier(0x100, 6, 0); __builtin_amdgcn_sched_group_barrier(0x8, 8, 0); }
    else if (i < 15) { __builtin_amdgcn_sched_group_barrier(0x100, 6, 0); __builtin_amdgcn_sched_group_barrier(0x8, 4, 0); }
  }
  __syncthreads();
  float* red = (float*)(sm + CV_UC);
#pragma unroll
  for (int i = 0; i < 16; ++i) {
    const int row = (i & 3) + 8 * (i >> 2) + 4 * h;
    red[(w * 2 + 0) * 1056 + r * 33 + row] = acc0[i];
    red[(w * 2 + 1) * 1056 + r * 33 + row] = acc1[i];
  }
  __syncthreads();
  const float dsk = bf2f(Dskip[d]);
  const u16* urP = (const u16*)(sm + CV_UR);
#pragma unroll
  for (int k = 0; k < 4; ++k) {
    const int l = 2 * (tid + 256 * k);
    float y[2];
#pragma unroll
    for (int e = 0; e < 2; ++e) {
      const int ll = l + e, a = ll >> 5, rr = ll & 31;
      const int tile = a >> 5, col = a & 31;
      float acc = 0.f;
#pragma unroll
      for (int ww = 0; ww < 4; ++ww) acc += red[(ww * 2 + tile) * 1056 + col * 33 + rr];
      const float uv = bf2f(urP[2063 - ll]);
      y[e] = rbf(acc) + rbf(uv * dsk);
    }
    *(unsigned*)(aT + (size_t)d * SL + l) = (unsigned)f2bf(y[0]) | ((unsigned)f2bf(y[1]) << 16);
  }
}

__global__ __launch_bounds__(256) void ln2_kernel(
    const u16* __restrict__ x, const u16* __restrict__ aT, u16* __restrict__ hbuf, u16* __restrict__ hn) {
  __shared__ __attribute__((aligned(16))) u16 tile[DM * 10];
  const int tid = threadIdx.x, w = tid >> 6, lane = tid & 63;
  const int l0 = blockIdx.x * 8;
  unsigned xv[2][5];
#pragma unroll
  for (int rr = 0; rr < 2; ++rr) {
    const unsigned* xr = (const unsigned*)(x + (size_t)(l0 + w * 2 + rr) * DM);
#pragma unroll
    for (int k = 0; k < 5; ++k) xv[rr][k] = xr[lane + 64 * k];
  }
  for (int dd = tid; dd < DM; dd += 256) {
    const uint4 v = *(const uint4*)(aT + (size_t)dd * SL + l0);
    unsigned* dst = (unsigned*)(tile + dd * 10);
    dst[0] = v.x; dst[1] = v.y; dst[2] = v.z; dst[3] = v.w;
  }
  __syncthreads();
  float v[2][10];
#pragma unroll
  for (int rr = 0; rr < 2; ++rr) {
    const int rl = w * 2 + rr;
#pragma unroll
    for (int k = 0; k < 5; ++k) {
      const unsigned p = xv[rr][k];
      const int d0 = 2 * (lane + 64 * k);
      v[rr][2 * k] = rbf(bf2f((u16)(p & 0xffff)) + bf2f(tile[d0 * 10 + rl]));
      v[rr][2 * k + 1] = rbf(bf2f((u16)(p >> 16)) + bf2f(tile[(d0 + 1) * 10 + rl]));
    }
    unsigned* hr = (unsigned*)(hbuf + (size_t)(l0 + rl) * DM);
#pragma unroll
    for (int k = 0; k < 5; ++k) hr[lane + 64 * k] = (unsigned)f2bf(v[rr][2 * k]) | ((unsigned)f2bf(v[rr][2 * k + 1]) << 16);
  }
#pragma unroll
  for (int pass = 0; pass < 2; ++pass) {
    float s[2], q[2];
#pragma unroll
    for (int rr = 0; rr < 2; ++rr) { s[rr] = 0.f;
#pragma unroll
      for (int k = 0; k < 10; ++k) s[rr] += v[rr][k]; }
#pragma unroll
    for (int o = 32; o >= 1; o >>= 1) { s[0] += __shfl_xor(s[0], o, 64); s[1] += __shfl_xor(s[1], o, 64); }
#pragma unroll
    for (int rr = 0; rr < 2; ++rr) { const float mean = s[rr] * (1.0f / DM); q[rr] = 0.f;
#pragma unroll
      for (int k = 0; k < 10; ++k) { float dlt = v[rr][k] - mean; q[rr] += dlt * dlt; } }
#pragma unroll
    for (int o = 32; o >= 1; o >>= 1) { q[0] += __shfl_xor(q[0], o, 64); q[1] += __shfl_xor(q[1], o, 64); }
#pragma unroll
    for (int rr = 0; rr < 2; ++rr) {
      const float mb = rbf(s[rr] * (1.0f / DM));
      const float den = rbf(sqrtf(rbf(rbf(q[rr] * (1.0f / DM)) + 1e-5f)));
#pragma unroll
      for (int k = 0; k < 10; ++k) v[rr][k] = rbf(rbf(v[rr][k] - mb) / den);
    }
  }
#pragma unroll
  for (int rr = 0; rr < 2; ++rr) {
    unsigned* nr = (unsigned*)(hn + (size_t)(l0 + w * 2 + rr) * DM);
#pragma unroll
    for (int k = 0; k < 5; ++k) nr[lane + 64 * k] = (unsigned)f2bf(v[rr][2 * k]) | ((unsigned)f2bf(v[rr][2 * k + 1]) << 16);
  }
}

#define WAIT_V(n) asm volatile("s_waitcnt vmcnt(" #n ")" ::: "memory")
__device__ __forceinline__ void glds16(const void* g, unsigned lds_dst) {
  unsigned keep;
  asm volatile("s_mov_b32 %0, m0\n\ts_mov_b32 m0, %2\n\ts_nop 0\n\tglobal_load_lds_dwordx4 %1, off\n\ts_mov_b32 m0, %0"
               : "=&s"(keep) : "v"(g), "s"(lds_dst) : "memory");
}
__device__ __forceinline__ unsigned lds_addr(const void* p) { return (unsigned)(size_t)p; }
__device__ __forceinline__ bf16x4 trread(const unsigned char* p) {
  return __builtin_amdgcn_ds_read_tr16_b64_v4i16((lds_bf16x4*)p);
}
__device__ __forceinline__ bf16x8 cat8(bf16x4 a, bf16x4 b) {
  bf16x8 r; r[0] = a[0]; r[1] = a[1]; r[2] = a[2]; r[3] = a[3]; r[4] = b[0]; r[5] = b[1]; r[6] = b[2]; r[7] = b[3]; return r;
}
__device__ __forceinline__ float gelu_tanh(float v) {
  const float k0 = 0.7978845608028654f, k1 = 0.044715f;
  float inner = k0 * (v + k1 * v * v * v);
  return 0.5f * v * (1.0f + tanhf(inner));
}

__global__ __launch_bounds__(256, 1) void gemm1_kernel(
    const u16* __restrict__ hn, const u16* __restrict__ w1, const u16* __restrict__ b1, u16* __restrict__ g) {
  __shared__ __attribute__((aligned(16))) unsigned char sm[4 * 4 * 8192];
  const int tid = threadIdx.x, w = tid >> 6, lane = tid & 63;
  const int xg = blockIdx.x & 7, li = blockIdx.x >> 3;
  const int nt = xg * 3 + li / 10, mt = li % 10;
  const int m0 = mt * 64, n0 = nt * 64;
  unsigned char* ring = sm + w * 4 * 8192;
  const unsigned ringa = __builtin_amdgcn_readfirstlane(lds_addr(ring));
  const int kbase = w * 512;
  const int srow = lane >> 3, sch = lane & 7;
  auto stage = [&](int it) {
    const unsigned dst = ringa + (it & 3) * 8192;
    const int k0 = kbase + it * 32;
#pragma unroll
    for (int i = 0; i < 4; ++i) {
      const int row = 8 * i + srow;
      const int ch = sch ^ (((row >> 1) & 1) << 2);
      glds16(hn + (size_t)(k0 + row) * DM + m0 + ch * 8, dst + i * 1024);
    }
#pragma unroll
    for (int i = 0; i < 4; ++i) {
      const int row = 8 * i + srow;
      const int ch = sch ^ (((row >> 1) & 1) << 2);
      glds16(w1 + (size_t)(k0 + row) * FF + n0 + ch * 8, dst + 4096 + i * 1024);
    }
  };
  f32x16 acc[2][2] = {};
  const int gq = lane >> 4, q = (lane & 15) >> 2, p = lane & 3;
  auto compute = [&](int it) {
    const unsigned char* buf = ring + (it & 3) * 8192;
#pragma unroll
    for (int ks = 0; ks < 2; ++ks) {
      bf16x8 af[2], bfr[2];
#pragma unroll
      for (int mi = 0; mi < 2; ++mi) {
        bf16x4 part[2];
#pragma unroll
        for (int t = 0; t < 2; ++t) {
          const int row = 16 * ks + 8 * (gq >> 1) + 4 * t + q;
          const int ch = (4 * mi + 2 * (gq & 1) + (p >> 1)) ^ (((row >> 1) & 1) << 2);
          part[t] = trread(buf + row * 128 + ch * 16 + 8 * (p & 1));
        }
        af[mi] = cat8(part[0], part[1]);
#pragma unroll
        for (int t = 0; t < 2; ++t) {
          const int row = 16 * ks + 8 * (gq >> 1) + 4 * t + q;
          const int ch = (4 * mi + 2 * (gq & 1) + (p >> 1)) ^ (((row >> 1) & 1) << 2);
          part[t] = trread(buf + 4096 + row * 128 + ch * 16 + 8 * (p & 1));
        }
        bfr[mi] = cat8(part[0], part[1]);
      }
#pragma unroll
      for (int mi = 0; mi < 2; ++mi)
#pragma unroll
        for (int ni = 0; ni < 2; ++ni)
          acc[mi][ni] = __builtin_amdgcn_mfma_f32_32x32x16_bf16(af[mi], bfr[ni], acc[mi][ni], 0, 0, 0);
    }
  };
  stage(0); stage(1); stage(2);
  for (int it = 0; it < 13; ++it) {
    stage(it + 3);
    WAIT_V(24);
    compute(it);
  }
  WAIT_V(16); compute(13);
  WAIT_V(8); compute(14);
  WAIT_V(0); compute(15);
  __syncthreads();
  float* red = (float*)sm;
  {
    const int h = lane >> 5, c = lane & 31;
#pragma unroll
    for (int mi = 0; mi < 2; ++mi)
#pragma unroll
      for (int ni = 0; ni < 2; ++ni)
#pragma unroll
        for (int i = 0; i < 16; ++i) {
          const int m = 32 * mi + (i & 3) + 8 * (i >> 2) + 4 * h;
          red[w * 4160 + m * 65 + 32 * ni + c] = acc[mi][ni][i];
        }
  }
  __syncthreads();
  {
    const int m = tid >> 2, nb = (tid & 3) * 16;
    u16 o[16];
#pragma unroll
    for (int j = 0; j < 16; ++j) {
      float s = 0.f;
#pragma unroll
      for (int ww = 0; ww < 4; ++ww) s += red[ww * 4160 + m * 65 + nb + j];
      const float v = rbf(rbf(s) + bf2f(b1[n0 + nb + j]));
      o[j] = f2bf(gelu_tanh(v));
    }
    uint4* dst = (uint4*)(g + (size_t)(m0 + m) * FF + n0 + nb);
    uint4 v0, v1;
    v0.x = o[0] | ((unsigned)o[1] << 16); v0.y = o[2] | ((unsigned)o[3] << 16); v0.z = o[4] | ((unsigned)o[5] << 16); v0.w = o[6] | ((unsigned)o[7] << 16);
    v1.x = o[8] | ((unsigned)o[9] << 16); v1.y = o[10] | ((unsigned)o[11] << 16); v1.z = o[12] | ((unsigned)o[13] << 16); v1.w = o[14] | ((unsigned)o[15] << 16);
    dst[0] = v0; dst[1] = v1;
  }
}

__global__ __launch_bounds__(256, 1) void gemm2_kernel(
    const u16* __restrict__ w2, const u16* __restrict__ g, const u16* __restrict__ b2,
    const u16* __restrict__ hbuf, u16* __restrict__ out) {
  __shared__ __attribute__((aligned(16))) unsigned char sm[4 * 4 * 9216];
  const int tid = threadIdx.x, w = tid >> 6, lane = tid & 63;
  const int xg = blockIdx.x & 7, li = blockIdx.x >> 3;
  const int st = xg * 4 + (li >> 3), dtile = li & 7;
  const int s0 = st * 64, d0 = dtile * 80;
  unsigned char* ring = sm + w * 4 * 9216;
  const unsigned ringa = __builtin_amdgcn_readfirstlane(lds_addr(ring));
  const int kbase = w * 384;
  auto stage = [&](int it) {
    const unsigned dst = ringa + (it & 3) * 9216;
    const int k0 = kbase + it * 32;
#pragma unroll
    for (int i = 0; i < 4; ++i) {
      const int row = 8 * i + (lane >> 3);
      const int ch = (lane & 7) ^ ((((row >> 1) & 1) << 1) | (((row >> 3) & 1) << 2));
      glds16(w2 + (size_t)(k0 + row) * SL + s0 + ch * 8, dst + i * 1024);
    }
#pragma unroll
    for (int i = 0; i < 5; ++i) {
      const int row = 16 * i + (lane >> 2);
      const int ch = (lane & 3) ^ ((row >> 1) & 3);
      glds16(g + (size_t)(d0 + row) * FF + k0 + ch * 8, dst + 4096 + i * 1024);
    }
  };
  f32x4 acc[4][5] = {};
  const int gq = lane >> 4, q = (lane & 15) >> 2, p = lane & 3, fr = lane & 15;
  auto compute = [&](int it) {
    const unsigned char* buf = ring + (it & 3) * 9216;
    bf16x8 af[4], bfr[5];
#pragma unroll
    for (int mi = 0; mi < 4; ++mi) {
      bf16x4 part[2];
#pragma unroll
      for (int t = 0; t < 2; ++t) {
        const int row = 8 * gq + 4 * t + q;
        const int ch = (2 * mi + (p >> 1)) ^ ((((row >> 1) & 1) << 1) | (((row >> 3) & 1) << 2));
        part[t] = trread(buf + row * 128 + ch * 16 + 8 * (p & 1));
      }
      af[mi] = cat8(part[0], part[1]);
    }
#pragma unroll
    for (int ni = 0; ni < 5; ++ni) {
      const int row = 16 * ni + fr;
      bfr[ni] = *(const bf16x8*)(buf + 4096 + row * 64 + 16 * (gq ^ ((row >> 1) & 3)));
    }
#pragma unroll
    for (int mi = 0; mi < 4; ++mi)
#pragma unroll
      for (int ni = 0; ni < 5; ++ni)
        acc[mi][ni] = __builtin_amdgcn_mfma_f32_16x16x32_bf16(af[mi], bfr[ni], acc[mi][ni], 0, 0, 0);
  };
  stage(0); stage(1); stage(2);
  for (int it = 0; it < 9; ++it) {
    stage(it + 3);
    WAIT_V(27);
    compute(it);
  }
  WAIT_V(18); compute(9);
  WAIT_V(9); compute(10);
  WAIT_V(0); compute(11);
  __syncthreads();
  float* red = (float*)sm;
#pragma unroll
  for (int mi = 0; mi < 4; ++mi)
#pragma unroll
    for (int ni = 0; ni < 5; ++ni)
#pragma unroll
      for (int r = 0; r < 4; ++r)
        red[w * 5184 + (16 * mi + 4 * gq + r) * 81 + 16 * ni + fr] = acc[mi][ni][r];
  __syncthreads();
  {
    const int m = tid >> 2, nb = (tid & 3) * 20;
    const float bb = bf2f(b2[s0 + m]);
    const u16* hr = hbuf + (size_t)(s0 + m) * DM + d0 + nb;
    u16* orow = out + (size_t)(s0 + m) * DM + d0 + nb;
#pragma unroll
    for (int j = 0; j < 20; j += 2) {
      float s_0 = 0.f, s_1 = 0.f;
#pragma unroll
      for (int ww = 0; ww < 4; ++ww) { s_0 += red[ww * 5184 + m * 81 + nb + j]; s_1 += red[ww * 5184 + m * 81 + nb + j + 1]; }
      const unsigned hv = *(const unsigned*)(hr + j);
      const float y0 = rbf(rbf(s_0) + bb), y1 = rbf(rbf(s_1) + bb);
      const u16 o0 = f2bf(bf2f((u16)(hv & 0xffff)) + y0), o1 = f2bf(bf2f((u16)(hv >> 16)) + y1);
      *(unsigned*)(orow + j) = (unsigned)o0 | ((unsigned)o1 << 16);
    }
  }
}

extern "C" void kernel_launch(void* const* d_in, const int* in_sizes, int n_in,
                              void* d_out, int out_size, void* d_ws, size_t ws_size,
                              hipStream_t stream) {
  const u16* x     = (const u16*)d_in[0];
  const u16* logA  = (const u16*)d_in[1];
  const u16* Aim   = (const u16*)d_in[2];
  const u16* Cre   = (const u16*)d_in[3];
  const u16* Cim   = (const u16*)d_in[4];
  const u16* logdt = (const u16*)d_in[5];
  const u16* Dskip = (const u16*)d_in[6];
  const u16* w1    = (const u16*)d_in[8];
  const u16* b1    = (const u16*)d_in[9];
  const u16* w2    = (const u16*)d_in[10];
  const u16* b2    = (const u16*)d_in[11];
  unsigned char* ws = (unsigned char*)d_ws;
  u16* Khi = (u16*)(ws + 16384);
  u16* Klo = Khi + (size_t)DM * SL;
  u16* aT  = Klo + (size_t)DM * SL;
  u16* hb  = aT + (size_t)DM * SL;
  u16* hn  = hb + (size_t)DM * SL;
  u16* g   = hn + (size_t)DM * SL;
  u16* urG = g + (size_t)DM * FF;
  u16* out = (u16*)d_out;
  prep_kernel<<<DM + 128, 256, 0, stream>>>(x, logA, Aim, Cre, Cim, logdt, urG, Khi, Klo);
  conv_kernel<<<DM, 256, 0, stream>>>(urG, Khi, Klo, Dskip, aT);
  ln2_kernel<<<SL / 8, 256, 0, stream>>>(x, aT, hb, hn);
  gemm1_kernel<<<240, 256, 0, stream>>>(hn, w1, b1, g);
  gemm2_kernel<<<256, 256, 0, stream>>>(w2, g, b2, hb, out);
}
```
